```python
import torch

def _softmax(x, axis=-1):
    return torch.softmax(x, dim=axis)
def _gelu(x):
    return torch.nn.functional.gelu(x, approximate="tanh")
def _silu(x):
    return torch.nn.functional.silu(x)
def _sigmoid(x):
    return torch.sigmoid(x)
def _layernorm(x, axis=-1, eps=1e-5):
    m = x.mean(dim=axis, keepdim=True)
    v = x.var(dim=axis, keepdim=True, unbiased=False)
    return (x - m) / torch.sqrt(v + eps)
def _scatter_add(tgt, idx, src, axis=0):
    return tgt.clone().index_add_(axis, idx, src)
def _segment_sum(src, seg, n):
    out = torch.zeros((n,) + src.shape[1:], dtype=src.dtype)
    return out.index_add_(0, seg, src)
def _pad(x, pw):
    flat = tuple(v for pair in reversed(pw) for v in pair)
    return torch.nn.functional.pad(x, flat)

kernel_name = 'standalone__paged_block_table_kv_pagedattention__sampling'

def setup_inputs():

    _w = (lambda a: (lambda t: t.to(torch.bfloat16) if t.is_floating_point() else t)(torch.as_tensor(a)))
    B, Hq, Hkv, Sq, Skv, D = 8, 32, 8, 8, 8192, 128
    bs = 16
    nbps = max(1, Skv // bs)
    num_phys = B * nbps + 4
    bt = torch.randperm(num_phys)[: B * nbps].reshape(B, nbps).to(torch.int64)
    cl = (nbps * bs - (torch.arange(1, B + 1, dtype=torch.int64) * 2654435761) % max(1, nbps * bs // 2)).to(torch.int64)
    return {
        "q": _w((lambda *s: torch.randn(*s, dtype=torch.bfloat16) * 0.8)(B, Hq, Sq, D)),
        "k_cache": _w((lambda *s: torch.randn(*s, dtype=torch.bfloat16) * 0.8)(num_phys, bs, Hkv, D)),
        "v_cache": _w((lambda *s: torch.randn(*s, dtype=torch.bfloat16) * 0.05)(num_phys, bs, Hkv, D)),
        "block_table": _w(bt),
        "context_lens": _w(cl),
    }

def reference(q, k_cache, v_cache, block_table, context_lens):
    B, Hq, Sq, D = q.shape
    bs = k_cache.shape[1]
    Hkv = k_cache.shape[2]
    nbps = block_table.shape[1]
    Skv = nbps * bs
    bt = block_table.reshape(-1)
    k = (lambda x, *d: x.permute(*d))(k_cache[bt].reshape(B, Skv, Hkv, D), 0, 2, 1, 3)
    v = (lambda x, *d: x.permute(*d))(v_cache[bt].reshape(B, Skv, Hkv, D), 0, 2, 1, 3)
    g = Hq // Hkv
    qg = q.reshape(B, Hkv, g, Sq, D)
    scores = torch.einsum("bhgqd,bhkd->bhgqk", qg, k) * (D ** -0.5)
    pos = torch.arange(Skv)
    msk = pos[None, :] < context_lens[:, None]
    scores = torch.where(msk[:, None, None, None, :], scores, -1e9)
    p = _softmax(scores, axis=-1)
    out = torch.einsum("bhgqk,bhkd->bhgqd", p, v)
    return out.reshape(B, Hq, Sq, D)
```

```cpp
#define RING 2
#include <hip/hip_runtime.h>
#include <hip/hip_bf16.h>
#include <stdint.h>

#define KV_AUX 2

namespace {

constexpr int B = 8, HKV = 8, D = 128, PAGE = 16, NBPS = 512, SKV = NBPS * PAGE;
constexpr int ROWS = 32;
constexpr int NW = 8;
constexpr int WGH = 32;
constexpr int W = NW * WGH;
constexpr int SLOTS = WGH + B;
constexpr unsigned PAGE_BYTES = PAGE * HKV * D * 2;
constexpr unsigned OOB = 0x7ff00000u;
constexpr float C2 = 0.08838834764831845f * 1.4426950408889634f;
constexpr float NEG = -1.0e30f;

constexpr int LROW = 132;
constexpr int LWAVE = ROWS * LROW;
constexpr int LDS_ML = NW * LWAVE;
constexpr int LDS_FLOATS = LDS_ML + 2 * NW * ROWS;

using bf16x2 = __attribute__((ext_vector_type(2))) __bf16;
using bf16x8 = __attribute__((ext_vector_type(8))) __bf16;
using u32x4 = __attribute__((ext_vector_type(4))) unsigned;
using u32x2 = __attribute__((ext_vector_type(2))) unsigned;
using f32x4 = __attribute__((ext_vector_type(4))) float;

__device__ __forceinline__ f32x4 mfma(u32x4 a, u32x4 b, f32x4 c) {
  return __builtin_amdgcn_mfma_f32_16x16x32_bf16(__builtin_bit_cast(bf16x8, a), __builtin_bit_cast(bf16x8, b), c, 0, 0, 0);
}
__device__ __forceinline__ unsigned pack_bf16(float lo, float hi) {
  bf16x2 v = {(__bf16)lo, (__bf16)hi};
  return __builtin_bit_cast(unsigned, v);
}
__device__ __forceinline__ float xg_max(float x) {
  auto s = __builtin_amdgcn_permlane16_swap(__float_as_uint(x), __float_as_uint(x), false, false);
  x = fmaxf(__uint_as_float(s[0]), __uint_as_float(s[1]));
  auto t = __builtin_amdgcn_permlane32_swap(__float_as_uint(x), __float_as_uint(x), false, false);
  return fmaxf(__uint_as_float(t[0]), __uint_as_float(t[1]));
}
__device__ __forceinline__ float xg_sum(float x) {
  auto s = __builtin_amdgcn_permlane16_swap(__float_as_uint(x), __float_as_uint(x), false, false);
  x = __uint_as_float(s[0]) + __uint_as_float(s[1]);
  auto t = __builtin_amdgcn_permlane32_swap(__float_as_uint(x), __float_as_uint(x), false, false);
  return __uint_as_float(t[0]) + __uint_as_float(t[1]);
}

struct Split { int len, pages, start; };
__device__ __forceinline__ Split split_work(const int* __restrict__ lens, int lane) {
  int cl = lens[lane & (B - 1)];
  int len = cl <= 0 ? SKV : min(cl, SKV);
  int pages = lane < B ? (len + PAGE - 1) / PAGE : 0;
  int inc = pages;
  inc += __builtin_amdgcn_update_dpp(0, inc, 0x111, 0xf, 0xf, true);
  inc += __builtin_amdgcn_update_dpp(0, inc, 0x112, 0xf, 0xf, true);
  inc += __builtin_amdgcn_update_dpp(0, inc, 0x114, 0xf, 0xf, true);
  const int total = __builtin_amdgcn_readlane(inc, B - 1);
  const float share = (float)(W - B) * (float)(inc - pages) * (1.0f / (float)total);
  int start = (lane & (B - 1)) + (int)share;
  if (lane >= B) start = W;
  return Split{cl <= 0 ? -len : len, pages, start};
}

}

__global__ __launch_bounds__(NW * 64, 2) void paged_attn_split(const __hip_bfloat16* __restrict__ q, const __hip_bfloat16* __restrict__ kc,
                                                               const __hip_bfloat16* __restrict__ vc, const int* __restrict__ bt,
                                                               const int* __restrict__ lens, float* __restrict__ po,
                                                               float* __restrict__ pml, unsigned cache_bytes) {
  __shared__ __attribute__((aligned(16))) float lds[LDS_FLOATS];
  const int tid = threadIdx.x, lane = tid & 63, wave = __builtin_amdgcn_readfirstlane(tid >> 6);
  const int h = blockIdx.x & (HKV - 1), wg = blockIdx.x >> 3;
  const int g = lane >> 4, c = lane & 15;

  const Split sp = split_work(lens, lane);
  const int slot = wg * NW + wave;
  const unsigned long long le = __ballot(lane >= 1 && lane < B && sp.start <= slot);
  const int b = __popcll(le);
  const int s0 = __builtin_amdgcn_readlane(sp.start, b), s1 = __builtin_amdgcn_readlane(sp.start, b + 1);
  const int pages = __builtin_amdgcn_readlane(sp.pages, b);
  const int slen = __builtin_amdgcn_readlane(sp.len, b);
  const int len = slen < 0 ? -slen : slen;
  const int n = s1 - s0, idx = slot - s0;
  const float ppw = (float)pages * (1.0f / (float)n);
  const int p0 = __builtin_amdgcn_readfirstlane((int)(ppw * (float)idx));
  const int p1 = idx + 1 == n ? pages : __builtin_amdgcn_readfirstlane((int)(ppw * (float)(idx + 1)));
  const int np = p1 - p0, ntile = (np + 1) >> 1;

  int btv = 0;
  if (lane < np) btv = bt[b * NBPS + p0 + lane];

  const auto qrs = __builtin_amdgcn_make_buffer_rsrc((void*)q, 0, B * HKV * ROWS * D * 2, 0x00020000);
  const auto krs = __builtin_amdgcn_make_buffer_rsrc((void*)kc, 0, cache_bytes, 0x00020000);
  const auto vrs = __builtin_amdgcn_make_buffer_rsrc((void*)vc, 0, cache_bytes, 0x00020000);

  u32x4 qf[2][2][2];
  {
    const unsigned qbase = slen < 0 ? OOB : (unsigned)(b * HKV + h) * (ROWS * D * 2);
    const unsigned qvo = c * 256 + g * 32;
#pragma unroll
    for (int qt = 0; qt < 2; ++qt)
#pragma unroll
      for (int hh = 0; hh < 2; ++hh)
#pragma unroll
        for (int bb = 0; bb < 2; ++bb)
          qf[qt][hh][bb] = __builtin_amdgcn_raw_buffer_load_b128(qrs, qvo + hh * 128 + bb * 16, qbase + qt * 4096, 0);
  }

  const unsigned kvo = (c >> 1) * 2048 + (2 * g + (c & 1)) * 16;
  const unsigned vvo = 2 * g * 2048 + c * 16;

  auto page_base = [&](int pi) -> unsigned {
    const unsigned phys = (unsigned)__builtin_amdgcn_readlane(btv, pi & 63);
    return pi < np ? phys * PAGE_BYTES + h * 256u : OOB;
  };
  auto issue_k = [&](int t, u32x4* r) {
#pragma unroll
    for (int pg = 0; pg < 2; ++pg) {
      const unsigned base = page_base(2 * t + pg);
#pragma unroll
      for (int x = 0; x < 4; ++x)
        r[pg * 4 + x] = __builtin_amdgcn_raw_buffer_load_b128(krs, kvo + (x & 1) * 128, base + (x >> 1) * 16384, KV_AUX);
    }
  };
  auto issue_v = [&](int t, u32x4* r) {
#pragma unroll
    for (int pg = 0; pg < 2; ++pg) {
      const unsigned base = page_base(2 * t + pg);
#pragma unroll
      for (int x = 0; x < 4; ++x)
        r[pg * 4 + x] = __builtin_amdgcn_raw_buffer_load_b128(vrs, vvo + (x & 1) * 2048, base + (x >> 1) * 16384, KV_AUX);
    }
  };

  f32x4 acc[8][2];
#pragma unroll
  for (int dd = 0; dd < 8; ++dd) { acc[dd][0] = f32x4{0, 0, 0, 0}; acc[dd][1] = f32x4{0, 0, 0, 0}; }
  float m[2] = {NEG, NEG}, l[2] = {0.f, 0.f};
  const int tok0 = p0 * PAGE + 2 * g;
  const int live_end = min(len, p1 * PAGE);

  u32x4 ring[RING][8];

  auto qk_softmax = [&](int t, const u32x4* kr, u32x4 (&pf)[2], float (&alpha)[2]) {
    float s[2][8];
#pragma unroll
    for (int gm = 0; gm < 4; ++gm) {
      f32x4 d[2][2];
#pragma unroll
      for (int qt = 0; qt < 2; ++qt)
#pragma unroll
        for (int bb = 0; bb < 2; ++bb) d[qt][bb] = mfma(kr[2 * gm], qf[qt][0][bb], f32x4{0, 0, 0, 0});
#pragma unroll
      for (int qt = 0; qt < 2; ++qt)
#pragma unroll
        for (int bb = 0; bb < 2; ++bb) d[qt][bb] = mfma(kr[2 * gm + 1], qf[qt][1][bb], d[qt][bb]);
#pragma unroll
      for (int qt = 0; qt < 2; ++qt)
#pragma unroll
        for (int u = 0; u < 2; ++u) s[qt][2 * gm + u] = d[qt][0][2 * u] + d[qt][1][2 * u + 1];
    }
    if ((t + 1) * 2 * PAGE + p0 * PAGE > live_end) {
      const int rem = live_end - (tok0 + t * 2 * PAGE);
#pragma unroll
      for (int j = 0; j < 8; ++j) {
        const bool ok = 8 * (j >> 1) + (j & 1) < rem;
        s[0][j] = ok ? s[0][j] : NEG;
        s[1][j] = ok ? s[1][j] : NEG;
      }
    }
#pragma unroll
    for (int qt = 0; qt < 2; ++qt) {
      float mx = fmaxf(fmaxf(fmaxf(s[qt][0], s[qt][1]), fmaxf(s[qt][2], s[qt][3])), fmaxf(fmaxf(s[qt][4], s[qt][5]), fmaxf(s[qt][6], s[qt][7])));
      mx = xg_max(mx);
      const float mn = fmaxf(m[qt], mx);
      alpha[qt] = __builtin_amdgcn_exp2f((m[qt] - mn) * C2);
      m[qt] = mn;
      const float mc = -mn * C2;
      float p[8], sum = 0.f;
#pragma unroll
      for (int j = 0; j < 8; ++j) { p[j] = __builtin_amdgcn_exp2f(fmaf(s[qt][j], C2, mc)); sum += p[j]; }
      l[qt] = fmaf(l[qt], alpha[qt], sum);
#pragma unroll
      for (int x = 0; x < 4; ++x) pf[qt][x] = pack_bf16(p[2 * x], p[2 * x + 1]);
    }
  };
  auto pv = [&](const u32x4* vr, const u32x4 (&pf)[2], const float (&alpha)[2]) {
#pragma unroll
    for (int dd = 0; dd < 8; ++dd) {
      u32x4 a;
#pragma unroll
      for (int x = 0; x < 4; ++x)
        a[x] = __builtin_amdgcn_perm(vr[2 * x + 1][dd >> 1], vr[2 * x][dd >> 1], (dd & 1) ? 0x07060302u : 0x05040100u);
      acc[dd][0] = mfma(a, pf[0], acc[dd][0] * alpha[0]);
      acc[dd][1] = mfma(a, pf[1], acc[dd][1] * alpha[1]);
    }
  };
#define ISSUE_HALF(n_, slot_) do { if ((((n_) & 1) == 0)) issue_k(((n_) >> 1) + tb, ring[slot_]); else issue_v(((n_) >> 1) + tb, ring[slot_]); asm volatile("" ::: "memory"); } while (0)
#define STEP(i_) do {                                                     \
    u32x4 pf[2]; float alpha[2];                                          \
    qk_softmax(tb + (i_), ring[(2 * (i_)) % RING], pf, alpha);            \
    __builtin_amdgcn_sched_barrier(0);                                    \
    ISSUE_HALF(2 * (i_) + RING, (2 * (i_)) % RING);                       \
    __builtin_amdgcn_sched_barrier(0);                                    \
    pv(ring[(2 * (i_) + 1) % RING], pf, alpha);                           \
    __builtin_amdgcn_sched_barrier(0);                                    \
    ISSUE_HALF(2 * (i_) + 1 + RING, (2 * (i_) + 1) % RING);               \
    __builtin_amdgcn_sched_barrier(0);                                    \
  } while (0)

  {
    int tb = 0;
#pragma unroll
    for (int x = 0; x < RING; ++x) ISSUE_HALF(x, x);
    __builtin_amdgcn_sched_barrier(0);
    constexpr int UNR = (RING & 1) ? RING : RING / 2;
    while (tb < ntile) {
      STEP(0);
      if constexpr (UNR > 1) { if (tb + 1 >= ntile) break; STEP(1); }
      if constexpr (UNR > 2) { if (tb + 2 >= ntile) break; STEP(2); }
      tb += UNR;
    }
  }
#undef STEP
#undef ISSUE_HALF

  l[0] = xg_sum(l[0]);
  l[1] = xg_sum(l[1]);
  {
    float* wl = lds + wave * LWAVE;
#pragma unroll
    for (int qt = 0; qt < 2; ++qt) {
#pragma unroll
      for (int r = 0; r < 4; ++r) {
        float* dst = wl + (qt * 16 + c) * LROW + 32 * g + 8 * r;
        *reinterpret_cast<f32x4*>(dst) = f32x4{acc[0][qt][r], acc[1][qt][r], acc[2][qt][r], acc[3][qt][r]};
        *reinterpret_cast<f32x4*>(dst + 4) = f32x4{acc[4][qt][r], acc[5][qt][r], acc[6][qt][r], acc[7][qt][r]};
      }
      if (g == 0) {
        lds[LDS_ML + wave * ROWS + qt * 16 + c] = m[qt];
        lds[LDS_ML + NW * ROWS + wave * ROWS + qt * 16 + c] = l[qt];
      }
    }
  }
  __syncthreads();
  {
    const int row = tid >> 4, d0 = (tid & 15) * 8;
    int w0 = 0;
    while (w0 < NW) {
      const int sl0 = wg * NW + w0;
      const int bs = __popcll(__ballot(lane >= 1 && lane < B && sp.start <= sl0));
      const int w1 = min(NW, __builtin_amdgcn_readlane(sp.start, bs + 1) - wg * NW);
      float M = NEG;
      for (int w = w0; w < w1; ++w) M = fmaxf(M, lds[LDS_ML + w * ROWS + row]);
      float L = 0.f;
      f32x4 o0 = {0, 0, 0, 0}, o1 = {0, 0, 0, 0};
      for (int w = w0; w < w1; ++w) {
        const float wt = __builtin_amdgcn_exp2f((lds[LDS_ML + w * ROWS + row] - M) * C2);
        L = fmaf(wt, lds[LDS_ML + NW * ROWS + w * ROWS + row], L);
        const float* src = lds + w * LWAVE + row * LROW + d0;
        o0 += wt * *reinterpret_cast<const f32x4*>(src);
        o1 += wt * *reinterpret_cast<const f32x4*>(src + 4);
      }
      const int ps = h * SLOTS + wg + bs;
      float* dst = po + ((size_t)ps * ROWS + row) * D + d0;
      *reinterpret_cast<f32x4*>(dst) = o0;
      *reinterpret_cast<f32x4*>(dst + 4) = o1;
      if ((tid & 15) == 0) *reinterpret_cast<float2*>(pml + ((size_t)ps * ROWS + row) * 2) = make_float2(M, L);
      w0 = w1;
    }
  }
}

__global__ __launch_bounds__(256) void paged_attn_combine(const int* __restrict__ lens, const float* __restrict__ po,
                                                          const float* __restrict__ pml, __hip_bfloat16* __restrict__ out) {
  const int tid = threadIdx.x, lane = tid & 63;
  const int h = blockIdx.x & (HKV - 1), b = (blockIdx.x >> 3) & (B - 1), rq = blockIdx.x >> 6;
  const Split sp = split_work(lens, lane);
  const int c0 = __builtin_amdgcn_readlane(sp.start, b) / NW, c1 = (__builtin_amdgcn_readlane(sp.start, b + 1) - 1) / NW;
  const int row = rq * 8 + (tid >> 5), d0 = (tid & 31) * 4;
  float M = NEG;
  for (int cc = c0; cc <= c1; ++cc) M = fmaxf(M, pml[((size_t)(h * SLOTS + cc + b) * ROWS + row) * 2]);
  float L = 0.f;
  f32x4 o = {0, 0, 0, 0};
  for (int cc = c0; cc <= c1; ++cc) {
    const size_t ps = (size_t)(h * SLOTS + cc + b) * ROWS + row;
    const float2 ml = *reinterpret_cast<const float2*>(pml + ps * 2);
    const float wt = __builtin_amdgcn_exp2f((ml.x - M) * C2);
    L = fmaf(wt, ml.y, L);
    o += wt * *reinterpret_cast<const f32x4*>(po + ps * D + d0);
  }
  const float inv = 1.0f / L;
  u32x2 r = {pack_bf16(o[0] * inv, o[1] * inv), pack_bf16(o[2] * inv, o[3] * inv)};
  *reinterpret_cast<u32x2*>(reinterpret_cast<char*>(out) + ((size_t)(b * HKV + h) * ROWS + row) * (D * 2) + d0 * 2) = r;
}

extern "C" void kernel_launch(void* const* d_in, const int* in_sizes, int n_in, void* d_out, int out_size, void* d_ws, size_t ws_size,
                              hipStream_t stream) {
  const __hip_bfloat16* q = (const __hip_bfloat16*)d_in[0];
  const __hip_bfloat16* kc = (const __hip_bfloat16*)d_in[1];
  const __hip_bfloat16* vc = (const __hip_bfloat16*)d_in[2];
  const int* bt = (const int*)d_in[3];
  const int* lens = (const int*)d_in[4];
  float* po = (float*)d_ws;
  float* pml = po + (size_t)HKV * SLOTS * ROWS * D;
  const unsigned cache_bytes = (unsigned)in_sizes[1] * 2u;
  paged_attn_split<<<HKV * WGH, NW * 64, 0, stream>>>(q, kc, vc, bt, lens, po, pml, cache_bytes);
  paged_attn_combine<<<HKV * B * 4, 256, 0, stream>>>(lens, po, pml, (__hip_bfloat16*)d_out);
}
```

```cpp
#include <hip/hip_runtime.h>
#include <hip/hip_bf16.h>
#include <stdint.h>

#define STAMP(w_, i_)
#define STAMP2(w_, i_)
#define TSTAMP(w_, i_)
#define ST_AUX 17
#define DIAG 0
#define LOCKSTEP 0
#define RING 2
#define KV_AUX 2

namespace {

constexpr int B = 8, HKV = 8, D = 128, PAGE = 16, NBPS = 512, SKV = NBPS * PAGE;
constexpr int ROWS = 32;
constexpr int NW = 8;
constexpr int HPW = 2;
constexpr int NWH = NW / HPW;
constexpr int WGH = 256 / (HKV / HPW);
constexpr int W = NWH * WGH;
constexpr int SLOTS = WGH + B;
constexpr unsigned PAGE_BYTES = PAGE * HKV * D * 2;
constexpr unsigned OOB = 0x7ff00000u;
constexpr float C2 = 0.08838834764831845f * 1.4426950408889634f;
constexpr float NEG = -1.0e30f;

constexpr int LROW = 132;
constexpr int LWAVE = ROWS * LROW;
constexpr int LDS_ML = NW * LWAVE;
constexpr int LDS_FLOATS = LDS_ML + 2 * NW * ROWS;

using bf16x2 = __attribute__((ext_vector_type(2))) __bf16;
using bf16x8 = __attribute__((ext_vector_type(8))) __bf16;
using u32x4 = __attribute__((ext_vector_type(4))) unsigned;
using u32x2 = __attribute__((ext_vector_type(2))) unsigned;
using f32x4 = __attribute__((ext_vector_type(4))) float;

__device__ __forceinline__ f32x4 mfma(u32x4 a, u32x4 b, f32x4 c) {
  return __builtin_amdgcn_mfma_f32_16x16x32_bf16(__builtin_bit_cast(bf16x8, a), __builtin_bit_cast(bf16x8, b), c, 0, 0, 0);
}
__device__ __forceinline__ unsigned pack_bf16(float lo, float hi) {
  bf16x2 v = {(__bf16)lo, (__bf16)hi};
  return __builtin_bit_cast(unsigned, v);
}
__device__ __forceinline__ float xg_max(float x) {
  auto s = __builtin_amdgcn_permlane16_swap(__float_as_uint(x), __float_as_uint(x), false, false);
  x = fmaxf(__uint_as_float(s[0]), __uint_as_float(s[1]));
  auto t = __builtin_amdgcn_permlane32_swap(__float_as_uint(x), __float_as_uint(x), false, false);
  return fmaxf(__uint_as_float(t[0]), __uint_as_float(t[1]));
}
__device__ __forceinline__ float xg_sum(float x) {
  auto s = __builtin_amdgcn_permlane16_swap(__float_as_uint(x), __float_as_uint(x), false, false);
  x = __uint_as_float(s[0]) + __uint_as_float(s[1]);
  auto t = __builtin_amdgcn_permlane32_swap(__float_as_uint(x), __float_as_uint(x), false, false);
  return __uint_as_float(t[0]) + __uint_as_float(t[1]);
}

struct Split { int len, pages, start; };
__device__ __forceinline__ Split split_work(const int* __restrict__ lens, int lane) {
  int cl = lens[lane & (B - 1)];
  int len = cl <= 0 ? SKV : min(cl, SKV);
  int pages = lane < B ? (len + PAGE - 1) / PAGE : 0;
  int inc = pages;
  inc += __builtin_amdgcn_update_dpp(0, inc, 0x111, 0xf, 0xf, true);
  inc += __builtin_amdgcn_update_dpp(0, inc, 0x112, 0xf, 0xf, true);
  inc += __builtin_amdgcn_update_dpp(0, inc, 0x114, 0xf, 0xf, true);
  const int total = __builtin_amdgcn_readlane(inc, B - 1);
  const float share = (float)(W - B) * (float)(inc - pages) * (1.0f / (float)total);
  int start = (lane & (B - 1)) + (int)share;
  if (lane >= B) start = W;
  return Split{cl <= 0 ? -len : len, pages, start};
}

}

__global__ __launch_bounds__(NW * 64, 2) void paged_attn_split(const __hip_bfloat16* __restrict__ q, const __hip_bfloat16* __restrict__ kc,
                                                               const __hip_bfloat16* __restrict__ vc, const int* __restrict__ bt,
                                                               const int* __restrict__ lens, float* __restrict__ po,
                                                               float* __restrict__ pml, unsigned cache_bytes) {
  __shared__ __attribute__((aligned(16))) float lds[LDS_FLOATS];
  const int tid = threadIdx.x, lane = tid & 63, wave = __builtin_amdgcn_readfirstlane(tid >> 6);
  const int h = HPW * (blockIdx.x & (HKV / HPW - 1)) + (wave & (HPW - 1)), wg = blockIdx.x / (HKV / HPW);
  const int g = lane >> 4, c = lane & 15;
  const int gw = blockIdx.x * NW + wave;
  STAMP(gw, 0);

  const Split sp = split_work(lens, lane);
  const int slot = wg * NWH + wave / HPW;
  const unsigned long long le = __ballot(lane >= 1 && lane < B && sp.start <= slot);
  const int b = __popcll(le);
  const int s0 = __builtin_amdgcn_readlane(sp.start, b), s1 = __builtin_amdgcn_readlane(sp.start, b + 1);
  const int pages = __builtin_amdgcn_readlane(sp.pages, b);
  const int slen = __builtin_amdgcn_readlane(sp.len, b);
  const int len = slen < 0 ? -slen : slen;
  const int n = s1 - s0, idx = slot - s0;
  const float ppw = (float)pages * (1.0f / (float)n);
  const int p0 = __builtin_amdgcn_readfirstlane((int)(ppw * (float)idx));
  const int p1 = idx + 1 == n ? pages : __builtin_amdgcn_readfirstlane((int)(ppw * (float)(idx + 1)));
  const int np = p1 - p0;
  const int ntile = (np + 1) >> 1;

  STAMP(gw, 1);
  int btv = 0;
  if (lane < np) btv = bt[b * NBPS + p0 + lane];

  const auto qrs = __builtin_amdgcn_make_buffer_rsrc((void*)q, 0, B * HKV * ROWS * D * 2, 0x00020000);
  const auto krs = __builtin_amdgcn_make_buffer_rsrc((void*)kc, 0, cache_bytes, 0x00020000);
  const auto vrs = __builtin_amdgcn_make_buffer_rsrc((void*)vc, 0, cache_bytes, 0x00020000);

  u32x4 qf[2][2][2];
  {
    const unsigned qbase = slen < 0 ? OOB : (unsigned)(b * HKV + h) * (ROWS * D * 2);
    const unsigned qvo = c * 256 + g * 32;
#pragma unroll
    for (int qt = 0; qt < 2; ++qt)
#pragma unroll
      for (int hh = 0; hh < 2; ++hh)
#pragma unroll
        for (int bb = 0; bb < 2; ++bb)
          qf[qt][hh][bb] = __builtin_amdgcn_raw_buffer_load_b128(qrs, qvo + hh * 128 + bb * 16, qbase + qt * 4096, 0);
  }

  const unsigned kvo = (c >> 1) * 2048 + (2 * g + (c & 1)) * 16;
  const unsigned vvo = 2 * g * 2048 + c * 16;

  auto page_base = [&](int pi) -> unsigned {
    const unsigned phys = (unsigned)__builtin_amdgcn_readlane(btv, pi & 63);
    return pi < np ? phys * PAGE_BYTES + h * 256u : OOB;
  };
  auto issue_k = [&](int t, u32x4* r) {
#pragma unroll
    for (int pg = 0; pg < 2; ++pg) {
      const unsigned base = page_base(2 * t + pg);
#pragma unroll
      for (int x = 0; x < 4; ++x)
        r[pg * 4 + x] = __builtin_amdgcn_raw_buffer_load_b128(krs, kvo + (x & 1) * 128, base + (x >> 1) * 16384, KV_AUX);
    }
  };
  auto issue_v = [&](int t, u32x4* r) {
#pragma unroll
    for (int pg = 0; pg < 2; ++pg) {
      const unsigned base = page_base(2 * t + pg);
#pragma unroll
      for (int x = 0; x < 4; ++x)
        r[pg * 4 + x] = __builtin_amdgcn_raw_buffer_load_b128(vrs, vvo + (x & 1) * 2048, base + (x >> 1) * 16384, KV_AUX);
    }
  };

  f32x4 acc[8][2];
#pragma unroll
  for (int dd = 0; dd < 8; ++dd) { acc[dd][0] = f32x4{0, 0, 0, 0}; acc[dd][1] = f32x4{0, 0, 0, 0}; }
  float m[2] = {NEG, NEG}, l[2] = {0.f, 0.f};
  const int tok0 = p0 * PAGE + 2 * g;
  const int live_end = min(len, p1 * PAGE);

  u32x4 ring[RING][8];

  auto qk_softmax = [&](int t, const u32x4* kr, u32x4 (&pf)[2], float (&alpha)[2]) {
    float s[2][8];
#pragma unroll
    for (int gm = 0; gm < 4; ++gm) {
      f32x4 d[2][2];
#pragma unroll
      for (int qt = 0; qt < 2; ++qt)
#pragma unroll
        for (int bb = 0; bb < 2; ++bb) d[qt][bb] = mfma(kr[2 * gm], qf[qt][0][bb], f32x4{0, 0, 0, 0});
#pragma unroll
      for (int qt = 0; qt < 2; ++qt)
#pragma unroll
        for (int bb = 0; bb < 2; ++bb) d[qt][bb] = mfma(kr[2 * gm + 1], qf[qt][1][bb], d[qt][bb]);
#pragma unroll
      for (int qt = 0; qt < 2; ++qt)
#pragma unroll
        for (int u = 0; u < 2; ++u) s[qt][2 * gm + u] = d[qt][0][2 * u] + d[qt][1][2 * u + 1];
    }
    if ((t + 1) * 2 * PAGE + p0 * PAGE > live_end) {
      const int rem = live_end - (tok0 + t * 2 * PAGE);
#pragma unroll
      for (int j = 0; j < 8; ++j) {
        const bool ok = 8 * (j >> 1) + (j & 1) < rem;
        s[0][j] = ok ? s[0][j] : NEG;
        s[1][j] = ok ? s[1][j] : NEG;
      }
    }
#pragma unroll
    for (int qt = 0; qt < 2; ++qt) {
      float mx = fmaxf(fmaxf(fmaxf(s[qt][0], s[qt][1]), fmaxf(s[qt][2], s[qt][3])), fmaxf(fmaxf(s[qt][4], s[qt][5]), fmaxf(s[qt][6], s[qt][7])));
      mx = xg_max(mx);
      const float mn = fmaxf(m[qt], mx);
      alpha[qt] = __builtin_amdgcn_exp2f((m[qt] - mn) * C2);
      m[qt] = mn;
      const float mc = -mn * C2;
      float p[8], sum = 0.f;
#pragma unroll
      for (int j = 0; j < 8; ++j) { p[j] = __builtin_amdgcn_exp2f(fmaf(s[qt][j], C2, mc)); sum += p[j]; }
      l[qt] = fmaf(l[qt], alpha[qt], sum);
#pragma unroll
      for (int x = 0; x < 4; ++x) pf[qt][x] = pack_bf16(p[2 * x], p[2 * x + 1]);
    }
  };
  auto pv = [&](const u32x4* vr, const u32x4 (&pf)[2], const float (&alpha)[2]) {
#pragma unroll
    for (int dd = 0; dd < 8; ++dd) {
      u32x4 a;
#pragma unroll
      for (int x = 0; x < 4; ++x)
        a[x] = __builtin_amdgcn_perm(vr[2 * x + 1][dd >> 1], vr[2 * x][dd >> 1], (dd & 1) ? 0x07060302u : 0x05040100u);
      acc[dd][0] = mfma(a, pf[0], acc[dd][0] * alpha[0]);
      acc[dd][1] = mfma(a, pf[1], acc[dd][1] * alpha[1]);
    }
  };
#define ISSUE_HALF(n_, slot_) do { if ((((n_) & 1) == 0)) issue_k(((n_) >> 1) + tb, ring[slot_]); else issue_v(((n_) >> 1) + tb, ring[slot_]); asm volatile("" ::: "memory"); } while (0)
#define STEP(i_) do {                                                     \
    u32x4 pf[2]; float alpha[2];                                          \
    qk_softmax(tb + (i_), ring[(2 * (i_)) % RING], pf, alpha);            \
    __builtin_amdgcn_sched_barrier(0);                                    \
    ISSUE_HALF(2 * (i_) + RING, (2 * (i_)) % RING);                       \
    __builtin_amdgcn_sched_barrier(0);                                    \
    pv(ring[(2 * (i_) + 1) % RING], pf, alpha);                           \
    __builtin_amdgcn_sched_barrier(0);                                    \
    ISSUE_HALF(2 * (i_) + 1 + RING, (2 * (i_) + 1) % RING);               \
    __builtin_amdgcn_sched_barrier(0);                                    \
  } while (0)

  {
    int tb = 0;
    asm volatile("" :: "v"(btv));
    STAMP(gw, 2);
#pragma unroll
    for (int x = 0; x < RING; ++x) ISSUE_HALF(x, x);
    __builtin_amdgcn_sched_barrier(0);
    constexpr int UNR = (RING & 1) ? RING : RING / 2;
    while (tb < ntile) {
      STEP(0);
      if (tb == 0) STAMP(gw, 3);
      TSTAMP(gw, tb);
      if constexpr (UNR > 1) { if (tb + 1 >= ntile) break; STEP(1); }
      if constexpr (UNR > 2) { if (tb + 2 >= ntile) break; STEP(2); }
      tb += UNR;
    }
  }
#undef STEP
#undef ISSUE_HALF

  STAMP(gw, 4);
  l[0] = xg_sum(l[0]);
  l[1] = xg_sum(l[1]);
  {
    float* wl = lds + wave * LWAVE;
#pragma unroll
    for (int qt = 0; qt < 2; ++qt) {
#pragma unroll
      for (int r = 0; r < 4; ++r) {
        float* dst = wl + (qt * 16 + c) * LROW + 32 * g + 8 * r;
        *reinterpret_cast<f32x4*>(dst) = f32x4{acc[0][qt][r], acc[1][qt][r], acc[2][qt][r], acc[3][qt][r]};
        *reinterpret_cast<f32x4*>(dst + 4) = f32x4{acc[4][qt][r], acc[5][qt][r], acc[6][qt][r], acc[7][qt][r]};
      }
      if (g == 0) {
        lds[LDS_ML + wave * ROWS + qt * 16 + c] = m[qt];
        lds[LDS_ML + NW * ROWS + wave * ROWS + qt * 16 + c] = l[qt];
      }
    }
  }
  __syncthreads();
  STAMP(gw, 5);
  {
    const int row = tid >> 4, d0 = (tid & 15) * 4;
    const int h0 = h & ~(HPW - 1);
    const auto ors = __builtin_amdgcn_make_buffer_rsrc((void*)po, 0, HKV * SLOTS * ROWS * D * 4, 0x00020000);
    const auto mrs = __builtin_amdgcn_make_buffer_rsrc((void*)pml, 0, HKV * SLOTS * ROWS * 8, 0x00020000);
    int w0 = 0;
    while (w0 < NWH) {
      const int bs = __popcll(__ballot(lane >= 1 && lane < B && sp.start <= wg * NWH + w0));
      const int w1 = min(NWH, __builtin_amdgcn_readlane(sp.start, bs + 1) - wg * NWH);
#pragma unroll
      for (int hh = 0; hh < HPW; ++hh) {
        float mw[NWH], M = NEG;
#pragma unroll
        for (int w = 0; w < NWH; ++w) {
          mw[w] = (w >= w0 && w < w1) ? lds[LDS_ML + (w * HPW + hh) * ROWS + row] : NEG;
          M = fmaxf(M, mw[w]);
        }
        float L = 0.f;
        f32x4 o0 = {0, 0, 0, 0}, o1 = {0, 0, 0, 0};
#pragma unroll
        for (int w = 0; w < NWH; ++w) {
          if (w >= w0 && w < w1) {
            const float wt = __builtin_amdgcn_exp2f((mw[w] - M) * C2);
            L = fmaf(wt, lds[LDS_ML + NW * ROWS + (w * HPW + hh) * ROWS + row], L);
            const float* src = lds + (w * HPW + hh) * LWAVE + row * LROW + d0;
            o0 += wt * *reinterpret_cast<const f32x4*>(src);
            o1 += wt * *reinterpret_cast<const f32x4*>(src + 64);
          }
        }
        const unsigned ps = (h0 + hh) * SLOTS + wg + bs;
        __builtin_amdgcn_raw_buffer_store_b128(__builtin_bit_cast(u32x4, o0), ors, (row * D + d0) * 4, ps * (ROWS * D * 4), ST_AUX);
        __builtin_amdgcn_raw_buffer_store_b128(__builtin_bit_cast(u32x4, o1), ors, (row * D + d0) * 4 + 256, ps * (ROWS * D * 4), ST_AUX);
        if ((tid & 15) == 0) __builtin_amdgcn_raw_buffer_store_b64(u32x2{__float_as_uint(M), __float_as_uint(L)}, mrs, row * 8, ps * (ROWS * 8), ST_AUX);
      }
      w0 = w1;
    }
  }
  STAMP(gw, 6);
}

constexpr int CB = 12;
__global__ __launch_bounds__(256) void paged_attn_combine(const int* __restrict__ lens, const float* __restrict__ po,
                                                          const float* __restrict__ pml, __hip_bfloat16* __restrict__ out) {
  const int tid = threadIdx.x, lane = tid & 63;
  const int h = blockIdx.x & (HKV - 1), b = (blockIdx.x >> 3) & (B - 1), rq = blockIdx.x >> 6;
  STAMP2(blockIdx.x, 0);
  const Split sp = split_work(lens, lane);
  const int c0 = __builtin_amdgcn_readlane(sp.start, b) / NWH, c1 = (__builtin_amdgcn_readlane(sp.start, b + 1) - 1) / NWH;
  const int row = rq * 8 + (tid >> 5), d0 = (tid & 31) * 4;
  const auto ors = __builtin_amdgcn_make_buffer_rsrc((void*)po, 0, HKV * SLOTS * ROWS * D * 4, 0x00020000);
  const auto mrs = __builtin_amdgcn_make_buffer_rsrc((void*)pml, 0, HKV * SLOTS * ROWS * 8, 0x00020000);
  const unsigned ovo = (row * D + d0) * 4, mvo = row * 8;
  float M = NEG, L = 0.f;
  f32x4 o = {0, 0, 0, 0};
  for (int cb = c0; cb <= c1; cb += CB) {
    u32x2 ml[CB];
    u32x4 x[CB];
#pragma unroll
    for (int k = 0; k < CB; ++k) {
      const unsigned ps = h * SLOTS + cb + k + b;
      const bool ok = cb + k <= c1;
      ml[k] = __builtin_amdgcn_raw_buffer_load_b64(mrs, mvo, ok ? ps * (ROWS * 8) : OOB, 0);
      x[k] = __builtin_amdgcn_raw_buffer_load_b128(ors, ovo, ok ? ps * (ROWS * D * 4) : OOB, 0);
    }
    float Mn = M;
#pragma unroll
    for (int k = 0; k < CB; ++k) Mn = cb + k <= c1 ? fmaxf(Mn, __uint_as_float(ml[k][0])) : Mn;
    const float a = __builtin_amdgcn_exp2f((M - Mn) * C2);
    L *= a;
    o *= a;
    M = Mn;
#pragma unroll
    for (int k = 0; k < CB; ++k) {
      const float wt = cb + k <= c1 ? __builtin_amdgcn_exp2f((__uint_as_float(ml[k][0]) - M) * C2) : 0.f;
      L = fmaf(wt, __uint_as_float(ml[k][1]), L);
      o += wt * __builtin_bit_cast(f32x4, x[k]);
    }
  }
  const float inv = 1.0f / L;
  u32x2 r = {pack_bf16(o[0] * inv, o[1] * inv), pack_bf16(o[2] * inv, o[3] * inv)};
  *reinterpret_cast<u32x2*>(reinterpret_cast<char*>(out) + ((size_t)(b * HKV + h) * ROWS + row) * (D * 2) + d0 * 2) = r;
  STAMP2(blockIdx.x, 1);
}

extern "C" void kernel_launch(void* const* d_in, const int* in_sizes, int n_in, void* d_out, int out_size, void* d_ws, size_t ws_size,
                              hipStream_t stream) {
  const __hip_bfloat16* q = (const __hip_bfloat16*)d_in[0];
  const __hip_bfloat16* kc = (const __hip_bfloat16*)d_in[1];
  const __hip_bfloat16* vc = (const __hip_bfloat16*)d_in[2];
  const int* bt = (const int*)d_in[3];
  const int* lens = (const int*)d_in[4];
  float* po = (float*)d_ws;
  float* pml = po + (size_t)HKV * SLOTS * ROWS * D;
  const unsigned cache_bytes = (unsigned)in_sizes[1] * 2u;
  paged_attn_split<<<HKV / HPW * WGH, NW * 64, 0, stream>>>(q, kc, vc, bt, lens, po, pml, cache_bytes);
  paged_attn_combine<<<HKV * B * 4, 256, 0, stream>>>(lens, po, pml, (__hip_bfloat16*)d_out);
}
```

```cpp
#include <hip/hip_runtime.h>
#include <hip/hip_bf16.h>
#include <stdint.h>

namespace {

constexpr int B = 8, HKV = 8, D = 128, PAGE = 16, NBPS = 512, SKV = NBPS * PAGE;
constexpr int ROWS = 32;
constexpr int NW = 8;
constexpr int HPW = 2;
constexpr int NWH = NW / HPW;
constexpr int WGH = 256 / (HKV / HPW);
constexpr int W = NWH * WGH;
constexpr int SLOTS = WGH + B;
constexpr unsigned PAGE_BYTES = PAGE * HKV * D * 2;
constexpr unsigned OOB = 0x7ff00000u;
constexpr float C2 = 0.08838834764831845f * 1.4426950408889634f;
constexpr float NEG = -1.0e30f;
constexpr int AUX_STREAM = 2;
constexpr int AUX_THROUGH = 17;

constexpr int LROW = 128;
constexpr int LWAVE = ROWS * LROW;
constexpr int LDS_ML = NW * LWAVE;
constexpr int LDS_FLOATS = LDS_ML + 2 * NW * ROWS;
constexpr int WA = 20, WB = 5;
constexpr int CB = 12;
constexpr int PRIM = HKV * B * CB;
constexpr int NBLK = PRIM + HKV * SLOTS;
constexpr int BLK_O = ROWS * D * 2, BLK_ML = ROWS * 8;

using bf16x2 = __attribute__((ext_vector_type(2))) __bf16;
using bf16x8 = __attribute__((ext_vector_type(8))) __bf16;
using u32x4 = __attribute__((ext_vector_type(4))) unsigned;
using u32x2 = __attribute__((ext_vector_type(2))) unsigned;
using f32x4 = __attribute__((ext_vector_type(4))) float;

__device__ __forceinline__ f32x4 mfma(u32x4 a, u32x4 b, f32x4 c) {
  return __builtin_amdgcn_mfma_f32_16x16x32_bf16(__builtin_bit_cast(bf16x8, a), __builtin_bit_cast(bf16x8, b), c, 0, 0, 0);
}
__device__ __forceinline__ unsigned pack_bf16(float lo, float hi) {
  bf16x2 v = {(__bf16)lo, (__bf16)hi};
  return __builtin_bit_cast(unsigned, v);
}
__device__ __forceinline__ float max3(float a, float b, float c) {
  float r;
  asm("v_max3_f32 %0, %1, %2, %3" : "=v"(r) : "v"(a), "v"(b), "v"(c));
  return r;
}
__device__ __forceinline__ float xg_max(float x) {
  auto s = __builtin_amdgcn_permlane16_swap(__float_as_uint(x), __float_as_uint(x), false, false);
  x = fmaxf(__uint_as_float(s[0]), __uint_as_float(s[1]));
  auto t = __builtin_amdgcn_permlane32_swap(__float_as_uint(x), __float_as_uint(x), false, false);
  return fmaxf(__uint_as_float(t[0]), __uint_as_float(t[1]));
}
__device__ __forceinline__ float xg_sum(float x) {
  auto s = __builtin_amdgcn_permlane16_swap(__float_as_uint(x), __float_as_uint(x), false, false);
  x = __uint_as_float(s[0]) + __uint_as_float(s[1]);
  auto t = __builtin_amdgcn_permlane32_swap(__float_as_uint(x), __float_as_uint(x), false, false);
  return __uint_as_float(t[0]) + __uint_as_float(t[1]);
}

struct Split { int len, pages, start; };
__device__ __forceinline__ Split split_work(int cl, int lane) {
  int len = cl <= 0 ? SKV : min(cl, SKV);
  int pages = lane < B ? (len + PAGE - 1) >> 4 : 0;
  int inc = pages;
  inc += __builtin_amdgcn_update_dpp(0, inc, 0x111, 0xf, 0xf, true);
  inc += __builtin_amdgcn_update_dpp(0, inc, 0x112, 0xf, 0xf, true);
  inc += __builtin_amdgcn_update_dpp(0, inc, 0x114, 0xf, 0xf, true);
  const int total = __builtin_amdgcn_readlane(inc, B - 1);
  const float share = (float)(W - B) * (float)(inc - pages) * __builtin_amdgcn_rcpf((float)total);
  int start = (lane & (B - 1)) + (int)share;
  if (lane >= B) start = W;
  return Split{cl <= 0 ? -len : len, pages, start};
}

template <int K> struct Idx { static constexpr int value = K; };
template <int N, int K = 0, class F> __device__ __forceinline__ void static_for(F&& f) {
  if constexpr (K < N) { f(Idx<K>{}); static_for<N, K + 1>(f); }
}

}

__global__ __launch_bounds__(NW * 64, 2) void paged_attn_split(const __hip_bfloat16* __restrict__ q, const __hip_bfloat16* __restrict__ kc,
                                                               const __hip_bfloat16* __restrict__ vc, const int* __restrict__ bt,
                                                               const int* __restrict__ lens, unsigned* __restrict__ po,
                                                               float* __restrict__ pml, unsigned cache_bytes) {
  __shared__ __attribute__((aligned(16))) float lds[LDS_FLOATS];
  const int tid = threadIdx.x, lane = tid & 63, wave = __builtin_amdgcn_readfirstlane(tid >> 6);
  const int pair = blockIdx.x & (HKV / HPW - 1), wg = blockIdx.x / (HKV / HPW);
  const int h0 = HPW * pair, h = h0 + (wave & (HPW - 1));
  const int g = lane >> 4, c = lane & 15;

  uint64_t pc;
  int code_bytes;
  asm volatile("s_getpc_b64 %0\n.Lpaged_attn_pc:\n\ts_mov_b32 %1, .Lpaged_attn_end-.Lpaged_attn_pc" : "=s"(pc), "=s"(code_bytes));
  const unsigned code_touch =
      __builtin_amdgcn_raw_buffer_load_b32(__builtin_amdgcn_make_buffer_rsrc((void*)pc, 0, max(code_bytes, 0), 0x00020000), tid * 32, 0, 0);
  __builtin_amdgcn_sched_barrier(0);

  asm volatile("" ::"s"(q), "s"(kc), "s"(vc), "s"(po), "s"(pml), "s"(cache_bytes));
  const int cl = lens[lane & (B - 1)];
  const int bt_touch = bt[wave * NBPS + lane * 8];
  const int slot = wg * NWH + wave / HPW;
  const auto cumw = [](int x) { return WA * x + WB * ((0x64 >> (2 * (x & 3))) & 3); };
  const int cw0 = cumw(slot), cw1 = cumw(slot + 1);
  __builtin_amdgcn_sched_barrier(0);
  const Split sp = split_work(cl, lane);
  const auto seq_of = [&](int slot_) { return (int)__popcll(__ballot(lane >= 1 && lane < B && sp.start <= slot_)); };
  const int b = seq_of(slot);
  const int next = __builtin_amdgcn_update_dpp(0, sp.start, 0x101, 0xf, 0xf, true);
  const int cs = cumw(sp.start);
  const float ppw = (float)sp.pages * __builtin_amdgcn_rcpf((float)(cumw(next) - cs));
  int v0 = (int)(ppw * (float)(cw0 - cs)), v1 = slot + 1 == next ? sp.pages : (int)(ppw * (float)(cw1 - cs));
  asm volatile("" : "+v"(v0), "+v"(v1));
  const int p0 = __builtin_amdgcn_readlane(v0, b), p1 = __builtin_amdgcn_readlane(v1, b);
  const int slen = __builtin_amdgcn_readlane(sp.len, b);
  const int len = slen < 0 ? -slen : slen;
  const int np = p1 - p0;
  const int ntile = (np + 1) >> 1;

  const auto brs = __builtin_amdgcn_make_buffer_rsrc((void*)bt, 0, B * NBPS * 4, 0x00020000);
  const int btv = __builtin_amdgcn_raw_buffer_load_b32(brs, lane * 4, (b * NBPS + p0) * 4, 0);

  const auto qrs = __builtin_amdgcn_make_buffer_rsrc((void*)q, 0, B * HKV * ROWS * D * 2, 0x00020000);
  const auto krs = __builtin_amdgcn_make_buffer_rsrc((void*)kc, 0, cache_bytes, 0x00020000);
  const auto vrs = __builtin_amdgcn_make_buffer_rsrc((void*)vc, 0, cache_bytes, 0x00020000);

  u32x4 qf[2][2][2];
  {
    const unsigned qbase = slen < 0 ? OOB : (unsigned)(b * HKV + h) * (ROWS * D * 2);
    const unsigned qvo = c * 256 + g * 32;
#pragma unroll
    for (int qt = 0; qt < 2; ++qt)
#pragma unroll
      for (int hh = 0; hh < 2; ++hh)
#pragma unroll
        for (int bb = 0; bb < 2; ++bb)
          qf[qt][hh][bb] = __builtin_amdgcn_raw_buffer_load_b128(qrs, qvo + hh * 128 + bb * 16, qbase + qt * 4096, 0);
    asm volatile("" ::: "memory");
    __builtin_amdgcn_sched_barrier(0);
  }

  const unsigned kvo = (c >> 1) * 2048 + (2 * g + (c & 1)) * 16;
  const unsigned vvo = 2 * g * 2048 + c * 16;

  auto page_base = [&](int pi) -> unsigned {
    const unsigned phys = (unsigned)__builtin_amdgcn_readlane(btv, pi & 63);
    return pi < np ? phys * PAGE_BYTES + h * 256u : OOB;
  };
  auto issue_k = [&](int t, u32x4* r) {
#pragma unroll
    for (int pg = 0; pg < 2; ++pg) {
      const unsigned base = page_base(2 * t + pg);
#pragma unroll
      for (int x = 0; x < 4; ++x)
        r[pg * 4 + x] = __builtin_amdgcn_raw_buffer_load_b128(krs, kvo + (x & 1) * 128, base + (x >> 1) * 16384, AUX_STREAM);
    }
    asm volatile("" ::: "memory");
  };
  auto issue_v = [&](int t, u32x4* r) {
#pragma unroll
    for (int pg = 0; pg < 2; ++pg) {
      const unsigned base = page_base(2 * t + pg);
#pragma unroll
      for (int x = 0; x < 4; ++x)
        r[pg * 4 + x] = __builtin_amdgcn_raw_buffer_load_b128(vrs, vvo + (x & 1) * 2048, base + (x >> 1) * 16384, AUX_STREAM);
    }
    asm volatile("" ::: "memory");
  };

  f32x4 acc[8][2];
#pragma unroll
  for (int dd = 0; dd < 8; ++dd) { acc[dd][0] = f32x4{0, 0, 0, 0}; acc[dd][1] = f32x4{0, 0, 0, 0}; }
  float m[2] = {NEG, NEG}, l[2] = {0.f, 0.f};
  const int tok0 = p0 * PAGE + 2 * g;
  const int live_end = min(len, p1 * PAGE);

  auto qk_softmax = [&](int t, bool mask, const u32x4* kr, u32x4 (&pf)[2], float (&alpha)[2]) {
    float s[2][8];
#pragma unroll
    for (int gm = 0; gm < 4; ++gm) {
      f32x4 d[2][2];
#pragma unroll
      for (int qt = 0; qt < 2; ++qt)
#pragma unroll
        for (int bb = 0; bb < 2; ++bb) d[qt][bb] = mfma(kr[2 * gm], qf[qt][0][bb], f32x4{0, 0, 0, 0});
#pragma unroll
      for (int qt = 0; qt < 2; ++qt)
#pragma unroll
        for (int bb = 0; bb < 2; ++bb) d[qt][bb] = mfma(kr[2 * gm + 1], qf[qt][1][bb], d[qt][bb]);
#pragma unroll
      for (int qt = 0; qt < 2; ++qt)
#pragma unroll
        for (int u = 0; u < 2; ++u) s[qt][2 * gm + u] = d[qt][0][2 * u] + d[qt][1][2 * u + 1];
    }
    if (mask) {
      const int rem = live_end - (tok0 + t * 2 * PAGE);
#pragma unroll
      for (int j = 0; j < 8; ++j) {
        const bool ok = 8 * (j >> 1) + (j & 1) < rem;
        s[0][j] = ok ? s[0][j] : NEG;
        s[1][j] = ok ? s[1][j] : NEG;
      }
    }
#pragma unroll
    for (int qt = 0; qt < 2; ++qt) {
      float mx = fmaxf(fmaxf(fmaxf(s[qt][0], s[qt][1]), fmaxf(s[qt][2], s[qt][3])), fmaxf(fmaxf(s[qt][4], s[qt][5]), fmaxf(s[qt][6], s[qt][7])));
      mx = xg_max(mx);
      const float mn = fmaxf(m[qt], mx);
      alpha[qt] = __builtin_amdgcn_exp2f((m[qt] - mn) * C2);
      m[qt] = mn;
      const float mc = -mn * C2;
      float p[8], sum = 0.f;
#pragma unroll
      for (int j = 0; j < 8; ++j) { p[j] = __builtin_amdgcn_exp2f(fmaf(s[qt][j], C2, mc)); sum += p[j]; }
      l[qt] = fmaf(l[qt], alpha[qt], sum);
#pragma unroll
      for (int x = 0; x < 4; ++x) pf[qt][x] = pack_bf16(p[2 * x], p[2 * x + 1]);
    }
  };
  auto pv = [&](const u32x4* vr, const u32x4 (&pf)[2], const float (&alpha)[2]) {
#pragma unroll
    for (int dd = 0; dd < 8; ++dd) {
      u32x4 a;
#pragma unroll
      for (int x = 0; x < 4; ++x)
        a[x] = __builtin_amdgcn_perm(vr[2 * x + 1][dd >> 1], vr[2 * x][dd >> 1], (dd & 1) ? 0x07060302u : 0x05040100u);
      acc[dd][0] = mfma(a, pf[0], acc[dd][0] * alpha[0]);
      acc[dd][1] = mfma(a, pf[1], acc[dd][1] * alpha[1]);
    }
  };

  u32x4 kr[8], vr[8];
  issue_k(0, kr);
  issue_v(0, vr);
  asm volatile("" ::"v"(bt_touch), "v"(code_touch));
  __builtin_amdgcn_sched_barrier(0);

  int blk[HPW][NWH];
  bool last[NWH];
  {
    int sq[NWH];
#pragma unroll
    for (int w = 0; w < NWH; ++w) sq[w] = seq_of(wg * NWH + w);
    const int k0 = wg - __builtin_amdgcn_readlane(sp.start, sq[0]) / NWH;
#pragma unroll
    for (int w = 0; w < NWH; ++w) {
      const int k = sq[w] == sq[0] ? k0 : 0;
      last[w] = w == NWH - 1 || sq[w] != sq[w < NWH - 1 ? w + 1 : w];
#pragma unroll
      for (int hh = 0; hh < HPW; ++hh) {
        blk[hh][w] = k < CB ? ((h0 + hh) * B + sq[w]) * CB + k : PRIM + (h0 + hh) * SLOTS + wg + sq[w];
        asm volatile("" : "+s"(blk[hh][w]));
      }
    }
  }

  const auto tile = [&](int t, auto more) {
    u32x4 pf[2];
    float alpha[2];
    qk_softmax(t, !more.value, kr, pf, alpha);
    __builtin_amdgcn_sched_barrier(0);
    if (more.value) issue_k(t + 1, kr);
    __builtin_amdgcn_sched_barrier(0);
    pv(vr, pf, alpha);
    __builtin_amdgcn_sched_barrier(0);
    if (more.value) issue_v(t + 1, vr);
    __builtin_amdgcn_sched_barrier(0);
  };
  for (int t = 0; t < ntile - 1; ++t) tile(t, Idx<1>{});
  if (ntile > 0) tile(ntile - 1, Idx<0>{});

  l[0] = xg_sum(l[0]);
  l[1] = xg_sum(l[1]);
  {
    float* wl = lds + wave * LWAVE + c * LROW;
    float* const wl2[2] = {wl + 4 * (g ^ (c & 7)), wl + 4 * ((g + 4) ^ (c & 7))};
#pragma unroll
    for (int qt = 0; qt < 2; ++qt) {
#pragma unroll
      for (int dd = 0; dd < 8; ++dd)
        *reinterpret_cast<f32x4*>(wl2[(dd >> 1) & 1] + qt * 16 * LROW + (dd >> 2) * 32 + (dd & 1) * 64) = acc[dd][qt];
      if (g == 0)
        *reinterpret_cast<float2*>(lds + LDS_ML + (((wave & (HPW - 1)) * ROWS + qt * 16 + c) * NWH + wave / HPW) * 2) = float2{m[qt], l[qt]};
    }
  }
  const int row = tid >> 4, j = tid & 15;
  unsigned src0 = (row * LROW + 4 * (j ^ (row & 7))) * 4, src1 = src0 + 4 * LWAVE * 4, mlo = (LDS_ML + row * (NWH * 2)) * 4;
  unsigned ovo = tid * 16, mvo = j == 0 ? row * 8 : OOB;
  asm volatile("" : "+v"(src0), "+v"(src1), "+v"(mlo), "+v"(ovo), "+v"(mvo));
  const auto ors = __builtin_amdgcn_make_buffer_rsrc((void*)po, 0, NBLK * BLK_O, 0x00020000);
  const auto mrs = __builtin_amdgcn_make_buffer_rsrc((void*)pml, 0, NBLK * BLK_ML, 0x00020000);
  __syncthreads();
  {
    const char* const ldsb = reinterpret_cast<const char*>(lds);
    f32x4 mlq[HPW][2], ow[HPW][NWH][2];
#pragma unroll
    for (int hh = 0; hh < HPW; ++hh) {
      mlq[hh][0] = *reinterpret_cast<const f32x4*>(ldsb + mlo + hh * (ROWS * NWH * 8));
      mlq[hh][1] = *reinterpret_cast<const f32x4*>(ldsb + mlo + hh * (ROWS * NWH * 8) + 16);
#pragma unroll
      for (int w = 0; w < NWH; ++w) {
        const int x = w * HPW + hh;
        const char* src = ldsb + (x < 4 ? src0 : src1) + (x & 3) * (LWAVE * 4);
        __builtin_amdgcn_sched_barrier(0);
        ow[hh][w][0] = *reinterpret_cast<const f32x4*>(src);
        ow[hh][w][1] = *reinterpret_cast<const f32x4*>(src + 256);
      }
      __builtin_amdgcn_sched_barrier(0);
    }
    const auto flush = [&](int blk_, float M_, float L_, const f32x4& o0, const f32x4& o1) {
      const u32x4 ob = {pack_bf16(o0[0], o1[0]), pack_bf16(o0[1], o1[1]), pack_bf16(o0[2], o1[2]), pack_bf16(o0[3], o1[3])};
      __builtin_amdgcn_raw_buffer_store_b128(ob, ors, ovo, blk_ * BLK_O, AUX_THROUGH);
      __builtin_amdgcn_raw_buffer_store_b64(u32x2{__float_as_uint(M_), __float_as_uint(L_)}, mrs, mvo, blk_ * BLK_ML, AUX_THROUGH);
    };
    if (!(last[0] || last[1] || last[2])) {
#pragma unroll
      for (int hh = 0; hh < HPW; ++hh) {
        const float M = max3(max3(mlq[hh][0][0], mlq[hh][0][2], mlq[hh][1][0]), mlq[hh][1][2], mlq[hh][1][2]);
        float L = 0.f;
        f32x4 o0, o1;
#pragma unroll
        for (int w = 0; w < NWH; ++w) {
          const float wt = __builtin_amdgcn_exp2f((mlq[hh][w >> 1][2 * (w & 1)] - M) * C2);
          L = w ? fmaf(wt, mlq[hh][w >> 1][2 * (w & 1) + 1], L) : wt * mlq[hh][0][1];
          o0 = w ? o0 + wt * ow[hh][w][0] : wt * ow[hh][w][0];
          o1 = w ? o1 + wt * ow[hh][w][1] : wt * ow[hh][w][1];
        }
        flush(blk[hh][NWH - 1], M, L, o0, o1);
      }
    } else {
#pragma unroll
      for (int hh = 0; hh < HPW; ++hh) {
        float mw[NWH], M[NWH];
#pragma unroll
        for (int w = 0; w < NWH; ++w) {
          mw[w] = mlq[hh][w >> 1][2 * (w & 1)];
          M[w] = w > 0 && !last[w > 0 ? w - 1 : 0] ? fmaxf(M[w > 0 ? w - 1 : 0], mw[w]) : mw[w];
        }
#pragma unroll
        for (int w = NWH - 2; w >= 0; --w) M[w] = last[w] ? M[w] : M[w + 1];
        float L = 0.f;
        f32x4 o0 = {0, 0, 0, 0}, o1 = {0, 0, 0, 0};
#pragma unroll
        for (int w = 0; w < NWH; ++w) {
          const float wt = __builtin_amdgcn_exp2f((mw[w] - M[w]) * C2);
          L = fmaf(wt, mlq[hh][w >> 1][2 * (w & 1) + 1], L);
          o0 += wt * ow[hh][w][0];
          o1 += wt * ow[hh][w][1];
          if (last[w]) {
            flush(blk[hh][w], M[w], L, o0, o1);
            L = 0.f;
            o0 = f32x4{0, 0, 0, 0};
            o1 = f32x4{0, 0, 0, 0};
          }
        }
      }
    }
  }
}

template <int CTRL> __device__ __forceinline__ float dpp(float x) {
  return __uint_as_float(__builtin_amdgcn_update_dpp(0, __float_as_uint(x), CTRL, 0xf, 0xf, true));
}
__global__ __launch_bounds__(128) void paged_attn_combine(const int* __restrict__ lens, const unsigned* __restrict__ po,
                                                          const float* __restrict__ pml, __hip_bfloat16* __restrict__ out) {
  const int tid = threadIdx.x, lane = tid & 63, c = tid & 15;
  const int h = blockIdx.x & (HKV - 1), b = (blockIdx.x >> 3) & (B - 1), rq = blockIdx.x >> 6;
  const int row = rq * 8 + (tid >> 4);
  const auto ors = __builtin_amdgcn_make_buffer_rsrc((void*)po, 0, NBLK * BLK_O, 0x00020000);
  const auto mrs = __builtin_amdgcn_make_buffer_rsrc((void*)pml, 0, NBLK * BLK_ML, 0x00020000);
  const unsigned ovo = row * (D * 2) + c * 16, mvo = row * 8 + c * BLK_ML;
  const int cl = lens[lane & (B - 1)];
  u32x2 ml;
  u32x4 x[CB];
  const auto fetch = [&](unsigned blk, int cnt) {
    ml = __builtin_amdgcn_raw_buffer_load_b64(mrs, c < cnt ? mvo : OOB, blk * BLK_ML, 0);
#pragma unroll
    for (int k = 0; k < CB; ++k) x[k] = __builtin_amdgcn_raw_buffer_load_b128(ors, ovo, k < cnt ? (blk + k) * BLK_O : OOB, 0);
  };
  fetch((h * B + b) * CB, CB);
  __builtin_amdgcn_sched_barrier(0);
  const Split sp = split_work(cl, lane);
  const int c0 = __builtin_amdgcn_readlane(sp.start, b) / NWH;
  const int n = (__builtin_amdgcn_readlane(sp.start, b + 1) - 1) / NWH - c0 + 1;
  float M = NEG, L = 0.f;
  float o[8] = {0, 0, 0, 0, 0, 0, 0, 0};
  for (int kb = 0;;) {
    const int cnt = min(n - kb, CB);
    const float mk = c < cnt ? __uint_as_float(ml[0]) : NEG;
    float Mn = fmaxf(mk, dpp<0x128>(mk));
    Mn = fmaxf(Mn, dpp<0x124>(Mn));
    Mn = fmaxf(Mn, dpp<0x122>(Mn));
    Mn = fmaxf(Mn, dpp<0x121>(Mn));
    Mn = fmaxf(Mn, M);
    const float wt = c < cnt ? __builtin_amdgcn_exp2f((mk - Mn) * C2) : 0.f;
    float ls = c < cnt ? wt * __uint_as_float(ml[1]) : 0.f;
    ls += dpp<0x128>(ls);
    ls += dpp<0x124>(ls);
    ls += dpp<0x122>(ls);
    ls += dpp<0x121>(ls);
    const float a = __builtin_amdgcn_exp2f((M - Mn) * C2);
    M = Mn;
    L = fmaf(L, a, ls);
#pragma unroll
    for (int j = 0; j < 8; ++j) o[j] *= a;
    const auto add = [&](auto kc) {
      constexpr int k = decltype(kc)::value;
      if (k < cnt) {
        const float w = dpp<0x150 + k>(wt);
#pragma unroll
        for (int j = 0; j < 4; ++j) {
          o[2 * j] = fmaf(w, __uint_as_float(x[k][j] << 16), o[2 * j]);
          o[2 * j + 1] = fmaf(w, __uint_as_float(x[k][j] & 0xffff0000u), o[2 * j + 1]);
        }
      }
    };
    static_for<CB>(add);
    kb += CB;
    if (kb >= n) break;
    fetch(PRIM + h * SLOTS + c0 + b + kb, n - kb);
  }
  const float inv = 1.0f / L;
  const auto rrs = __builtin_amdgcn_make_buffer_rsrc((void*)out, 0, B * HKV * ROWS * D * 2, 0x00020000);
  const unsigned rvo = row * (D * 2) + (c & 3) * 64 + (c >> 2) * 4, rso = (b * HKV + h) * (ROWS * D * 2);
#pragma unroll
  for (int j = 0; j < 4; ++j) __builtin_amdgcn_raw_buffer_store_b32(pack_bf16(o[2 * j] * inv, o[2 * j + 1] * inv), rrs, rvo + 16 * j, rso, 0);
  asm volatile(".Lpaged_attn_end:");
}

extern "C" void kernel_launch(void* const* d_in, const int* in_sizes, int n_in, void* d_out, int out_size, void* d_ws, size_t ws_size,
                              hipStream_t stream) {
  const __hip_bfloat16* q = (const __hip_bfloat16*)d_in[0];
  const __hip_bfloat16* kc = (const __hip_bfloat16*)d_in[1];
  const __hip_bfloat16* vc = (const __hip_bfloat16*)d_in[2];
  const int* bt = (const int*)d_in[3];
  const int* lens = (const int*)d_in[4];
  unsigned* po = (unsigned*)d_ws;
  float* pml = (float*)(po + (size_t)NBLK * BLK_O / 4);
  const unsigned cache_bytes = (unsigned)in_sizes[1] * 2u;
  paged_attn_split<<<HKV / HPW * WGH, NW * 64, 0, stream>>>(q, kc, vc, bt, lens, po, pml, cache_bytes);
  paged_attn_combine<<<HKV * B * 4, 128, 0, stream>>>(lens, po, pml, (__hip_bfloat16*)d_out);
}
```
